```python
import torch

def _softmax(x, axis=-1):
    return torch.softmax(x, dim=axis)
def _gelu(x):
    return torch.nn.functional.gelu(x, approximate="tanh")
def _silu(x):
    return torch.nn.functional.silu(x)
def _sigmoid(x):
    return torch.sigmoid(x)
def _layernorm(x, axis=-1, eps=1e-5):
    m = x.mean(dim=axis, keepdim=True)
    v = x.var(dim=axis, keepdim=True, unbiased=False)
    return (x - m) / torch.sqrt(v + eps)
def _scatter_add(tgt, idx, src, axis=0):
    return tgt.clone().index_add_(axis, idx, src)
def _segment_sum(src, seg, n):
    out = torch.zeros((n,) + src.shape[1:], dtype=src.dtype)
    return out.index_add_(0, seg, src)
def _pad(x, pw):
    flat = tuple(v for pair in reversed(pw) for v in pair)
    return torch.nn.functional.pad(x, flat)

kernel_name = 'concat_heads__linear_attn_block__softmax_full_mha__prompt'

def setup_inputs():

    _w = (lambda a: (lambda t: t.to(torch.bfloat16) if t.is_floating_point() else t)(torch.as_tensor(a)))
    def _A_setup():
        B, S, D = 1, 2048, 1024
        return {
            "x": _w((lambda *s: torch.randn(*s, dtype=torch.bfloat16) * 0.05)(B, S, D)),
            "wqkv": _w((lambda *s: torch.randn(*s, dtype=torch.bfloat16) * 1.6)(D, 3 * D)),
            "wo": _w((lambda *s: torch.randn(*s, dtype=torch.bfloat16) * 0.1)(D, D)),
        }
    _A_in = _A_setup()

    def _B_setup():
        B, S, D = 1, 2048, 1024
        return {
            "x": _w((lambda *s: torch.randn(*s, dtype=torch.bfloat16) * 0.2)(B, S, D)),
            "wqkv": _w((lambda *s: torch.randn(*s, dtype=torch.bfloat16) * 0.2)(D, 3 * D)),
            "wo": _w((lambda *s: torch.randn(*s, dtype=torch.bfloat16) * 0.05)(D, D)),
        }
    _B_in = _B_setup()

    return {
        "A_x": _A_in["x"],
        "A_wqkv": _A_in["wqkv"],
        "A_wo": _A_in["wo"],
        "B_x": _B_in["x"],
        "B_wqkv": _B_in["wqkv"],
        "B_wo": _B_in["wo"],
    }

def reference(A_x, A_wqkv, A_wo, B_x, B_wqkv, B_wo):
    def _A_ref(x, wqkv, wo):
        B, S, D = x.shape
        H = 32; Dh = D // H
        qkv = (lambda x, *d: x.permute(*d))((x @ wqkv).reshape(B, S, 3, H, Dh), 2, 0, 3, 1, 4)
        q, k, v = qkv[0], qkv[1], qkv[2]
        phi = lambda t: torch.where(t > 0, t + 1, torch.exp(t)) + 1e-6
        pq, pk = phi(q), phi(k)
        kv = torch.einsum("bhsd,bhse->bhde", pk, v)
        z = torch.einsum("bhsd->bhd", pk)
        num = torch.einsum("bhqd,bhde->bhqe", pq, kv)
        den = torch.einsum("bhqd,bhd->bhq", pq, z)[..., None] + 1e-6
        o = num / den
        return (lambda x, *d: x.permute(*d))(o, 0, 2, 1, 3).reshape(B, S, D) @ wo

    def _B_ref(x, wqkv, wo):
        B, S, D = x.shape
        H = 32; Dh = D // H
        qkv = (lambda x, *d: x.permute(*d))((x @ wqkv).reshape(B, S, 3, H, Dh), 2, 0, 3, 1, 4)
        q, k, v = qkv[0], qkv[1], qkv[2]
        scale = Dh ** -0.5
        scores = torch.einsum("bhqd,bhkd->bhqk", q, k) * scale
        p = _softmax(scores, axis=-1)
        o = torch.einsum("bhqk,bhkd->bhqd", p, v)
        return (lambda x, *d: x.permute(*d))(o, 0, 2, 1, 3).reshape(B, S, D) @ wo

    a = _A_ref(A_x, A_wqkv, A_wo)
    b = _B_ref(B_x, B_wqkv, B_wo)
    return torch.concatenate([a, b], axis=-1)
```

```cpp
#include <hip/hip_runtime.h>
#include <hip/hip_bf16.h>

typedef float f32x16 __attribute__((ext_vector_type(16)));
typedef float f32x8 __attribute__((ext_vector_type(8)));
typedef float f32x4 __attribute__((ext_vector_type(4)));
typedef float f32x2 __attribute__((ext_vector_type(2)));
typedef __bf16 bf16x2 __attribute__((ext_vector_type(2)));
typedef __bf16 bf16x8 __attribute__((ext_vector_type(8)));
typedef __bf16 bf16x16 __attribute__((ext_vector_type(16)));
typedef short s16x4 __attribute__((ext_vector_type(4)));
typedef unsigned u32x2 __attribute__((ext_vector_type(2)));
typedef unsigned u32x4 __attribute__((ext_vector_type(4)));
#define LAS __attribute__((address_space(3)))
#define GAS __attribute__((address_space(1)))
typedef LAS const char* lds_cp;
typedef LAS char* lds_p;
typedef GAS unsigned gu32;
typedef GAS unsigned long long gu64;
#define DI __device__ __forceinline__
#define SBAR() __builtin_amdgcn_sched_barrier(0)
#define PIN(x) asm volatile("" : "+v"(x))
#define RLX_AGENT __ATOMIC_RELAXED, __HIP_MEMORY_SCOPE_AGENT
#define NOP_ do {} while (0)

constexpr unsigned WS_Q = 0;
constexpr unsigned WS_KV = 4u << 20, KV_H = 262144 + 256;
constexpr unsigned WS_KVP = 49u << 18;
constexpr unsigned WS_ZP = 53u << 18;
constexpr unsigned WS_O = 54u << 18, O_TM = 524288 + 256;
constexpr unsigned WS_FL = 87u << 18;
constexpr unsigned WS_BYTES = 22u << 20;
#define HEARTBEAT (fl)
#define FLAG(isb, tm_, tn_, k) (fl + (1 + (isb) * 128 + (tm_) * 16 + (tn_)) * 32 + (k))
constexpr int LEADER = 247;
constexpr unsigned SPIN_CAP = 1u << 18;

constexpr float LOG2E = 1.4426950408889634f;
constexpr float QSCALE = 0.17677669529663687f * LOG2E;

DI f32x16 mfma32(bf16x8 a, bf16x8 b, f32x16 c) { return __builtin_amdgcn_mfma_f32_32x32x16_bf16(a, b, c, 0, 0, 0); }
DI f32x4 mfma16(bf16x8 a, bf16x8 b, f32x4 c) { return __builtin_amdgcn_mfma_f32_16x16x32_bf16(a, b, c, 0, 0, 0); }
DI unsigned cvtpk(float lo, float hi) { f32x2 v = {lo, hi}; return __builtin_bit_cast(unsigned, __builtin_convertvector(v, bf16x2)); }
DI u32x4 pack8(const f32x16& v, int s) { return (u32x4){cvtpk(v[8 * s], v[8 * s + 1]), cvtpk(v[8 * s + 2], v[8 * s + 3]), cvtpk(v[8 * s + 4], v[8 * s + 5]), cvtpk(v[8 * s + 6], v[8 * s + 7])}; }
DI u32x4 pack44(f32x4 a, f32x4 b) { return (u32x4){cvtpk(a[0], a[1]), cvtpk(a[2], a[3]), cvtpk(b[0], b[1]), cvtpk(b[2], b[3])}; }
DI bf16x8 asbf(u32x4 v) { return __builtin_bit_cast(bf16x8, v); }
DI bf16x8 ldsr128(lds_cp p) { return *(LAS const bf16x8*)p; }
DI s16x4 ldstr(lds_cp p) { return __builtin_amdgcn_ds_read_tr16_b64_v4i16((LAS s16x4*)p); }
DI bf16x8 cat8(s16x4 a, s16x4 b) { return __builtin_bit_cast(bf16x8, __builtin_shufflevector(a, b, 0, 1, 2, 3, 4, 5, 6, 7)); }
typedef __amdgpu_buffer_rsrc_t srd_t;
DI srd_t mksrd(const void* p, unsigned bytes) { return __builtin_amdgcn_make_buffer_rsrc((void*)p, 0, bytes, 0x00020000); }
DI void st_dev(srd_t srd, unsigned voff, unsigned soff, u32x4 v) { __builtin_amdgcn_raw_buffer_store_b128(v, srd, voff + soff, 0, 16); }
DI u32x4 ld_dev(srd_t srd, unsigned voff, unsigned soff) { return __builtin_amdgcn_raw_buffer_load_b128(srd, voff, soff, 16); }
template <bool DEV> DI void dma16(srd_t srd, unsigned voff, unsigned sb, unsigned sc, unsigned lb, unsigned lc) {
  unsigned t;
  if (DEV) asm volatile("s_add_u32 m0, %1, %2\n\ts_add_u32 %0, %3, %4\n\tbuffer_load_dwordx4 %5, %6, %0 offen sc1 lds" : "=&s"(t) : "s"(lb), "i"(lc), "s"(sb), "i"(sc), "v"(voff), "s"(srd) : "memory", "scc");
  else     asm volatile("s_add_u32 m0, %1, %2\n\ts_add_u32 %0, %3, %4\n\tbuffer_load_dwordx4 %5, %6, %0 offen lds" : "=&s"(t) : "s"(lb), "i"(lc), "s"(sb), "i"(sc), "v"(voff), "s"(srd) : "memory", "scc");
}
#define WAIT_BAR(N) asm volatile("s_waitcnt vmcnt(" #N ") lgkmcnt(0)\n\ts_barrier" ::: "memory")
#define DRAIN() asm volatile("s_waitcnt vmcnt(0)" ::: "memory")
DI float exp2_clamp(float u) { return __builtin_amdgcn_fmed3f(__builtin_amdgcn_exp2f(u), 0.f, 1.f); }
DI f32x4 phi4(f32x4 v) {
  const f32x4 u = v * LOG2E, a = v + 1e-6f;
  return (f32x4){__builtin_fmaxf(a[0], 1e-6f), __builtin_fmaxf(a[1], 1e-6f), __builtin_fmaxf(a[2], 1e-6f), __builtin_fmaxf(a[3], 1e-6f)} +
         (f32x4){exp2_clamp(u[0]), exp2_clamp(u[1]), exp2_clamp(u[2]), exp2_clamp(u[3])};
}
#define PIN4(a, b, c, d) asm volatile("" : "+v"(a), "+v"(b), "+v"(c), "+v"(d))
DI f32x4 phi4s(f32x4 v) {
  float u0 = v[0] * LOG2E, u1 = v[1] * LOG2E, u2 = v[2] * LOG2E, u3 = v[3] * LOG2E; PIN4(u0, u1, u2, u3);
  float a0 = v[0] + 1e-6f, a1 = v[1] + 1e-6f, a2 = v[2] + 1e-6f, a3 = v[3] + 1e-6f;
  a0 = __builtin_fmaxf(a0, 1e-6f); a1 = __builtin_fmaxf(a1, 1e-6f); a2 = __builtin_fmaxf(a2, 1e-6f); a3 = __builtin_fmaxf(a3, 1e-6f);
  a0 += exp2_clamp(u0); a1 += exp2_clamp(u1); a2 += exp2_clamp(u2); a3 += exp2_clamp(u3); PIN4(a0, a1, a2, a3);
  return (f32x4){a0, a1, a2, a3};
}
DI f32x4 add4s(f32x4 a, f32x4 b) { float b0 = b[0], b1 = b[1], b2 = b[2], b3 = b[3]; PIN4(b0, b1, b2, b3);
  b0 += a[0]; b1 += a[1]; b2 += a[2]; b3 += a[3]; PIN4(b0, b1, b2, b3); return (f32x4){b0, b1, b2, b3}; }
DI f32x4 mul4s(f32x4 a, float b) { float r0 = a[0] * b, r1 = a[1] * b, r2 = a[2] * b, r3 = a[3] * b; PIN4(r0, r1, r2, r3); return (f32x4){r0, r1, r2, r3}; }
DI void peek_a(gu32* p) { asm volatile("global_load_dword v254, %0, off sc1" :: "v"(p) : "memory", "v254"); }
DI void peek_b(gu32* p) { asm volatile("global_load_dword v253, %0, off sc1" :: "v"(p) : "memory", "v253"); }
DI unsigned long long seen_a() { unsigned long long m; asm volatile("v_cmp_ne_u32_e64 %0, 0, v254" : "=s"(m) :: "memory"); return m; }
DI unsigned long long seen_b() { unsigned long long m; asm volatile("v_cmp_ne_u32_e64 %0, 0, v253" : "=s"(m) :: "memory"); return m; }
DI void touch(srd_t srd, unsigned voff, unsigned soff) { asm volatile("buffer_load_dword v255, %0, %1, %2 offen" :: "v"(voff), "s"(srd), "s"(soff) : "memory", "v255"); }
constexpr unsigned OOB = 0x80000000u;
DI unsigned poll32(gu32* p) { unsigned v; asm volatile("global_load_dword %0, %1, off sc1\n\ts_waitcnt vmcnt(0)" : "=v"(v) : "v"(p) : "memory"); return v; }
#define SPIN _Pragma("clang loop unroll(disable)")
DI bool spin_step(unsigned& spins, gu32* fl, bool beat) {
  __builtin_amdgcn_s_sleep(2);
  ++spins;
  if (beat && (spins & 255u) == 0u && (threadIdx.x & 63) == 0) __hip_atomic_fetch_add(HEARTBEAT, 1u, RLX_AGENT);
  return spins > SPIN_CAP;
}

constexpr unsigned L1_X = 0, L1_WQK = 98304, L1_WV = 147456, LDS_BYTES = 163840;
constexpr unsigned SLOT = 32768;

struct F16 { bf16x8 x[4]; bf16x8 w[6]; };

constexpr int p3_first(bool B) { return B ? 6 : 16; }
constexpr int p3_cum(bool B, int t) {
  constexpr int a[17] = {0, 4, 8, 12, 15, 19, 23, 26, 30, 34, 37, 41, 45, 48, 48, 48, 48}, b[17] = {0, 2, 4, 6, 8, 10, 14, 18, 22, 25, 28, 32, 35, 38, 38, 38, 38};
  return B ? b[t] : a[t]; }
constexpr int p3_stage(bool B, int i) { return B && i < 14 ? (i < 6 ? 5 + i / 2 : 8 + ((i - 6) >> 1 & 1)) : i >= p3_first(B) ? 8 + (i - p3_first(B)) / 4 : i < 4 ? 3 + i / 2 : 5 + (i - 4) / 4; }
constexpr bool p3_isw(bool B, int i) { return B && i < 14 ? (i >= 6 && i < 10) : i >= p3_first(B) ? ((i - p3_first(B)) & 2) != 0 : i >= 4 && ((i - 4) & 2) == 0; }
constexpr int p3_need(bool B, int s) {
  return B && s == 8 ? 12 : s >= 8 ? p3_first(B) + 4 * (s - 7) : B ? (s >= 5 ? 2 * (s - 4) : -2 * (4 - s)) : s >= 5 ? 4 + 4 * (s - 4) : s >= 3 ? 2 * (s - 2) : 0; }

DI f32x16 smf32(bf16x8 a, f32x8 b, f32x16 c, int idx) { return __builtin_amdgcn_smfmac_f32_32x32x32_bf16(a, __builtin_bit_cast(bf16x16, b), c, idx, 0, 0); }
DI f32x4 smf16(bf16x8 a, f32x8 b, f32x4 c, int idx) { return __builtin_amdgcn_smfmac_f32_16x16x64_bf16(a, __builtin_bit_cast(bf16x16, b), c, idx, 0, 0); }
DI f32x8 lo8(const f32x16& v) { return __builtin_shufflevector(v, v, 0, 1, 2, 3, 4, 5, 6, 7); }
DI f32x8 hi8(const f32x16& v) { return __builtin_shufflevector(v, v, 8, 9, 10, 11, 12, 13, 14, 15); }
DI float max16(const f32x16& p) {
  float a = __builtin_fmaxf(__builtin_fmaxf(p[0], p[1]), p[2]), b = __builtin_fmaxf(__builtin_fmaxf(p[3], p[4]), p[5]);
#pragma unroll
  for (int i = 6; i < 14; i += 4) { a = __builtin_fmaxf(__builtin_fmaxf(a, p[i]), p[i + 1]); b = __builtin_fmaxf(__builtin_fmaxf(b, p[i + 2]), p[i + 3]); }
  a = __builtin_fmaxf(__builtin_fmaxf(a, p[14]), p[15]);
  const float m = __builtin_fmaxf(a, b);
  auto rr = __builtin_amdgcn_permlane32_swap(__float_as_uint(m), __float_as_uint(m), false, false);
  return __builtin_fmaxf(__uint_as_float(rr[0]), __uint_as_float(rr[1]));
}
DI f32x16 splat16(float v) { f32x16 r;
#pragma unroll
  for (int j = 0; j < 16; ++j) r[j] = v;
  return r; }

struct Entry {
  srd_t sx, sw; const char* wo; char* ws; char* out;
  unsigned vx0, vx1, vw, gx0, gw0, k3, gw, dx, dqk, dv, hb0, wlo;
};
#define SETK() do { gv = gw; gx = gx0 + k3 * 128u; gw = gw0 + k3 * 393216u; k3 = (k3 + 1u) & 15u; } while (0)
#define DMA_X(b3, p) dma16<false>(sx, ((p) & 1) ? vx1 : vx0, gx, (p) * 16384u, dx, (b3) * 32768u + (p) * 1024u)
#define DMA_WQK(b3, g) dma16<false>(sw, vw, gw, (g) * 2048u, dqk, (b3) * 16384u + (g) * 8192u)
#define DMA_WV(src, lc) dma16<false>(sw, vw, src, 4096u, dv, lc)

template <bool BRB>
DI void body(const Entry& e, char* lds) {
  char* const ws = e.ws; char* const out = e.out; const char* const wo = e.wo;
  const int xcd = blockIdx.x & 7, bk = blockIdx.x >> 4;
  const int tm = (xcd & 1) * 4 + (bk & 3), tn = (xcd >> 1) * 4 + (bk >> 2);
  bf16x8 pq[4];
  const int tid = threadIdx.x, lane = tid & 63, wid = __builtin_amdgcn_readfirstlane(tid >> 6);
  const int wm = wid & 3, wn = wid >> 2, c16 = lane & 15, g4 = lane >> 4, q4 = c16 >> 2, p4 = lane & 3;
  const unsigned lds0 = (unsigned)(uintptr_t)lds;
  gu32* fl = (gu32*)(ws + WS_FL);
  const bool leader = blockIdx.x == LEADER;
  const srd_t sws = mksrd(ws, WS_BYTES), sx = e.sx, sw = e.sw;
  const unsigned vx0 = e.vx0, vx1 = e.vx1, vw = e.vw, gx0 = e.gx0, gw0 = e.gw0, dx = e.dx, dqk = e.dqk, hb0 = e.hb0;
  unsigned k3 = e.k3, gw = e.gw, dv = e.dv, gx, gv;
  const lds_cp L = (lds_cp)lds;
  const unsigned ax = L1_X + (wm * 64 + c16) * 128 + ((g4 ^ ((c16 >> 1) & 7)) << 4);
  const unsigned aw = (8 * g4 + q4) * 128 + (((wn * 4 + (p4 >> 1)) ^ (((q4 >> 1) & 1) << 2) ^ ((g4 & 1) << 1)) << 4) + (p4 & 1) * 8;
  lds_cp x0l = L + ax, x1l = L + (ax ^ 64), x0h = x0l + 65536, x1h = x1l + 65536;
  lds_cp q0p = L + (L1_WQK + aw), q1p = L + (L1_WQK + (aw ^ 32)), v0p = L + (L1_WV + aw), v1p = L + (L1_WV + (aw ^ 32));
  PIN(x0l); PIN(x1l); PIN(x0h); PIN(x1h); PIN(q0p); PIN(q1p); PIN(v0p); PIN(v1p);
  f32x4 acc[4][6];
#pragma unroll
  for (int a = 0; a < 4; ++a)
#pragma unroll
    for (int c = 0; c < 6; ++c) acc[a][c] = (f32x4){};
  F16 FA, FB; s16x4 ha[6], hb[6];
#define MF(F, a, c) acc[a][c] = ((c) < 2 || (BRB && (c) < 4)) ? mfma16(F.w[c], F.x[a], acc[a][c]) : mfma16(F.x[a], F.w[c], acc[a][c]); SBAR()
#define XP(b3, s, a) (((b3) == 2 ? ((s) ? x1h : x0h) : ((s) ? x1l : x0l) + (b3) * 32768) + (a) * 2048)
#define WP(b3, s, c) (((c) < 4 ? (((c) & 1) ? q1p : q0p) + (b3) * 16384 + ((c) >> 1) * 8192 : (((c) & 1) ? v1p : v0p)) + (s) * 4096)
#define RX(R, N, b3, s, a) if (R) N.x[a] = ldsr128(XP(b3, s, a)); SBAR()
#define RA(R, b3, s, c) if (R) ha[c] = ldstr(WP(b3, s, c)); SBAR()
#define RB(R, b3, s, c) if (R) hb[c] = ldstr(WP(b3, s, c) + 512); SBAR()
#define TOGV() do { v0p = (lds_cp)((uintptr_t)v0p ^ 8192u); v1p = (lds_cp)((uintptr_t)v1p ^ 8192u); PIN(v0p); PIN(v1p); } while (0)
#define CATW(N, c0) do { _Pragma("unroll") for (int c_ = c0; c_ < 6; ++c_) N.w[c_] = cat8(ha[c_], hb[c_]); } while (0)
#define SUBA(F, N, b3) do { \
    MF(F,0,0); RX(1,N,b3,1,0); MF(F,1,0); RA(1,b3,1,0);   MF(F,2,0); RB(1,b3,1,0); MF(F,3,0); RX(1,N,b3,1,1); \
    MF(F,3,1); RX(1,N,b3,1,2); MF(F,2,1); RX(1,N,b3,1,3); MF(F,1,1); RA(1,b3,1,1); MF(F,0,1); RB(1,b3,1,1); \
    MF(F,0,2); RA(1,b3,1,2);   MF(F,1,2); RB(1,b3,1,2);   MF(F,2,2); RA(1,b3,1,3); MF(F,3,2); RB(1,b3,1,3); \
    MF(F,3,3); RA(1,b3,1,4);   MF(F,2,3); RB(1,b3,1,4);   MF(F,1,3); RA(1,b3,1,5); MF(F,0,3); RB(1,b3,1,5); \
    MF(F,0,4); TOGV(); SBAR(); MF(F,1,4); gv = gw; gx = gx0 + k3 * 128u; asm volatile("" : "+s"(gv), "+s"(gx)); SBAR(); \
    MF(F,2,4); gw = gw0 + k3 * 393216u; asm volatile("" : "+s"(gw)); SBAR(); MF(F,3,4); k3 = (k3 + 1u) & 15u; dv ^= 8192u; asm volatile("" : "+s"(k3), "+s"(dv)); SBAR(); \
    MF(F,3,5); MF(F,2,5); MF(F,1,5); MF(F,0,5); CATW(N, 0); } while (0)
#define SUBB(F, N, b3, HOOK) do { constexpr int n3 = ((b3) + 1) % 3; PRIO(1); \
    MF(F,0,0); RX(1,N,n3,0,0); MF(F,1,0); RA(1,n3,0,0);   MF(F,2,0); RB(1,n3,0,0); MF(F,3,0); DMA_WV(gv, 0); HOOK; SBAR(); \
    MF(F,3,1); RX(1,N,n3,0,1); MF(F,2,1); RX(1,N,n3,0,2); MF(F,1,1); DMA_WQK(b3, 0); SBAR(); MF(F,0,1); RX(1,N,n3,0,3); \
    MF(F,0,2); RA(1,n3,0,1);   MF(F,1,2); DMA_WQK(b3, 1); SBAR(); MF(F,2,2); RB(1,n3,0,1); MF(F,3,2); RA(1,n3,0,2); \
    MF(F,3,3); DMA_X(b3, 0); SBAR(); MF(F,2,3); RB(1,n3,0,2); MF(F,1,3); RA(1,n3,0,3); MF(F,0,3); DMA_X(b3, 1); SBAR(); \
    MF(F,0,4); RB(1,n3,0,3);   MF(F,1,4); RA(1,n3,0,4);   MF(F,2,4); DMA_X(b3, 2); SBAR(); MF(F,3,4); RB(1,n3,0,4); \
    MF(F,3,5); RA(1,n3,0,5);   MF(F,2,5); DMA_X(b3, 3); HELP_W(); SBAR(); MF(F,1,5); RB(1,n3,0,5); PRIO(0); MF(F,0,5); CATW(N, 0); } while (0)
#define PRIO(p) do { __builtin_amdgcn_s_setprio(p); SBAR(); } while (0)
  const unsigned jb = (3u - (e.wlo >> 7) - (tn & ~3)) & 7u, jn = tn & 3;
  const bool helper = jb < 4 && jn != jb;
  const unsigned vh = lane < 2 ? (((jn - (jn > jb)) * 4 + (tm & 3)) * 16 + wid * 2 + lane) * 2048 + ((tn & ~3) + jb) * 128 : OOB;
#define HELP_W() do { if (helper) touch(sw, vh, ((k3 + 1u) & 15u) * 393216u); } while (0)
#define STAGE(b3, HOOK) do { SUBA(FA, FB, b3); WAIT_BAR(6); SUBB(FB, FA, b3, HOOK); } while (0)
#define BEAT() do { if (!BRB && leader && wid == 0 && lane == 0) __hip_atomic_fetch_add(HEARTBEAT, 1u, RLX_AGENT); } while (0)
  WAIT_BAR(14);
#pragma unroll
  for (int a = 0; a < 4; ++a) FA.x[a] = ldsr128(XP(0, 0, a));
#pragma unroll
  for (int c = 0; c < 6; ++c) FA.w[c] = cat8(ldstr(WP(0, 0, c)), ldstr(WP(0, 0, c) + 512));
  STAGE(0, NOP_);
  if (!BRB && leader) {
    st_dev(sws, lane < 32 ? (1 + wid * 32 + lane) * 128 : OOB, WS_FL, (u32x4){});
  }
  for (int it = 0; it < 4; ++it) { STAGE(1, if (it) BEAT()); STAGE(2, NOP_); STAGE(0, NOP_); }
#define SUBKV(F, N, RV, cN, b3, s, E0) do { \
    MF(F,0,2); RX(1,N,b3,s,0); MF(F,1,2); RA(1,b3,s,cN); MF(F,2,2); RB(1,b3,s,cN);   MF(F,3,2); RX(1,N,b3,s,1); \
    MF(F,0,3); RX(1,N,b3,s,2); MF(F,1,3); RX(1,N,b3,s,3); MF(F,2,3); RA(1,b3,s,cN + 1); MF(F,3,3); RB(1,b3,s,cN + 1); E0; SBAR(); \
    MF(F,0,4); RA(RV,b3,s,4);  MF(F,1,4); RB(RV,b3,s,4); MF(F,2,4); RA(RV,b3,s,5);   MF(F,3,4); RB(RV,b3,s,5); \
    MF(F,0,5); MF(F,1,5); MF(F,2,5); MF(F,3,5); \
    N.w[cN] = cat8(ha[cN], hb[cN]); N.w[cN + 1] = cat8(ha[cN + 1], hb[cN + 1]); if (RV) { N.w[4] = cat8(ha[4], hb[4]); N.w[5] = cat8(ha[5], hb[5]); } } while (0)
#define SUB2(F, cF, N, R, cN, b3, s, H0, H1) do { \
    MF(F,0,cF); RX(R,N,b3,s,0); MF(F,1,cF); RA(R,b3,s,cN); MF(F,2,cF); RB(R,b3,s,cN); MF(F,3,cF); RX(R,N,b3,s,1); RX(R,N,b3,s,2); H0; SBAR(); \
    MF(F,0,cF + 1); RX(R,N,b3,s,3); MF(F,1,cF + 1); RA(R,b3,s,cN + 1); MF(F,2,cF + 1); RB(R,b3,s,cN + 1); MF(F,3,cF + 1); H1; SBAR(); \
    if (R) { N.w[cN] = cat8(ha[cN], hb[cN]); N.w[cN + 1] = cat8(ha[cN + 1], hb[cN + 1]); } } while (0)
#define QTAIL(H) do { SUB2(FA, 0, FB, 1, 0, 2, 0, H(0), H(1)); SUB2(FB, 0, FA, 1, 0, 2, 1, H(2), H(3)); SUB2(FA, 0, FB, 1, 0, 0, 0, H(4), H(5)); \
    SUB2(FB, 0, FA, 1, 0, 0, 1, H(6), H(7)); SUB2(FA, 0, FB, 0, 0, 0, 0, NOP_, NOP_); } while (0)
  unsigned hb1;
  SUBA(FA, FB, 1);
  WAIT_BAR(6);
  DMA_WV(gv, 0);
  const int head = tn * 2 + wn, s_ = g4 >> 1, h_ = g4 & 1;
  lds_p P = (lds_p)lds;
  int idx; bf16x8 sel; lds_cp c2, c3, c4; f32x16 Oa, Ob; f32x4 La, Lb;
#define P2_CONST() do { idx = (int)0xDDDDDDDDu;     \
    const unsigned sv_ = (lane & 3) ? 0u : 0x3f803f80u;        \
    sel = asbf((lane & 4) ? (u32x4){0u, 0u, sv_, sv_} : (u32x4){sv_, sv_, 0u, 0u}); \
    c2 = L + 2 * SLOT + wn * 16384 + lane * 16; c3 = c2 + SLOT; c4 = c2 + 2 * SLOT;        \
    Oa = (f32x16){}; Ob = (f32x16){}; La = (f32x4){}; Lb = (f32x4){}; \
    asm volatile("" : "+v"(Oa), "+v"(Ob), "+v"(La), "+v"(Lb), "+v"(sel), "+v"(idx), "+v"(c2), "+v"(c3), "+v"(c4)); } while (0)
#define HANDSHAKE() do { const unsigned hs0 = __builtin_amdgcn_readfirstlane(hb0); unsigned hs1 = __builtin_amdgcn_readfirstlane(hb1); asm volatile("" : "+s"(hs1)); \
    if (wid == 0 && !(!BRB && leader)) { unsigned spins = 0;        \
    SPIN while (hs1 == hs0) { if (spin_step(spins, fl, false)) break; hs1 = __builtin_amdgcn_readfirstlane(poll32(HEARTBEAT)); } } } while (0)
  if (BRB) {
    const unsigned gkv = WS_KV + (unsigned)head * KV_H + (unsigned)(tm * 8 + wm * 2) * 4096u, gq = WS_Q + (unsigned)(head * 64 + tm * 8 + wm * 2) * 2048u;
    const unsigned ok = s_ * 1024 + ((c16 >> 2) * 8 + (c16 & 3) + 32 * h_) * 16, ov = 2048 + s_ * 1024 + (c16 + 32 * h_) * 16;
    u32x4 pc[8];
#define HK(a) do { pc[a] = pack44(acc[a][2], acc[a][3]); st_dev(sws, ok, gkv + ((a) >> 1) * 4096 + ((a) & 1) * 64, pc[a]); } while (0)
#define HV(a) do { if ((a) < 4) { constexpr int a_ = (a) & 3; pc[4 + a_] = pack44(acc[a_ & 2][4 + (a_ & 1)], acc[(a_ & 2) + 1][4 + (a_ & 1)]); \
      st_dev(sws, ov, gkv + (a_ >> 1) * 4096 + (a_ & 1) * 256, pc[4 + a_]); } } while (0)
    SUBKV(FB, FA, 0, 2, 2, 0, NOP_);
    SUB2(FA, 2, FB, 1, 2, 2, 1, NOP_, NOP_);
    WAIT_BAR(1);
    hb1 = __hip_atomic_load(HEARTBEAT, RLX_AGENT);
    SUB2(FB, 2, FA, 1, 2, 0, 0, NOP_, NOP_);
    SUB2(FA, 2, FB, 1, 2, 0, 1, NOP_, NOP_);
    SUB2(FB, 2, FA, 1, 4, 2, 0, NOP_, NOP_);
    SUB2(FA, 4, FB, 1, 4, 2, 1, HK(0), HK(1));
    WAIT_BAR(3);
    TOGV(); SBAR();
    SUB2(FB, 4, FA, 1, 4, 0, 0, HK(2), HK(3));
    SUB2(FA, 4, FB, 1, 4, 0, 1, NOP_, NOP_);
    SUB2(FB, 4, FA, 1, 0, 1, 1, NOP_, NOP_);
    QTAIL(HV);
    u32x4 qc[4];
#pragma unroll
    for (int a = 0; a < 4; ++a) { qc[a] = pack44(acc[a][0] * QSCALE, acc[a][1] * QSCALE); if (wn) st_dev(sws, ov - 2048, gq + (a >> 1) * 2048 + (a & 1) * 256, qc[a]); }
    HANDSHAKE();
    asm volatile("s_barrier" ::: "memory");
    if (wn == 0) {
#pragma unroll
      for (int a = 0; a < 4; ++a) {
        *(LAS u32x4*)(P + 3 * SLOT + (wm * 2 + (a >> 1)) * 4096 + (a & 1) * 64 + ok) = pc[a];
        *(LAS u32x4*)(P + 3 * SLOT + (wm * 2 + (a >> 1)) * 4096 + (a & 1) * 256 + ov) = pc[4 + a];
        *(LAS u32x4*)(P + wm * 4096 + (a >> 1) * 2048 + (a & 1) * 256 + (ov - 2048)) = qc[a];
      }
    }
    P2_CONST();
    DRAIN();
    asm volatile("s_waitcnt lgkmcnt(0)\n\ts_barrier" ::: "memory");
  } else {
    u32x2 pkh[8];
#define HA(i) do { constexpr int tb = (i) >> 2, dt = ((i) >> 1) & 1; const f32x4 f = phi4s(acc[2 * tb + ((i) & 1)][2 + dt]); \
      pkh[i] = (u32x2){cvtpk(f[0], f[1]), cvtpk(f[2], f[3])}; PIN(pkh[i]); } while (0)
    SUBKV(FB, FA, 1, 2, 2, 0, NOP_);
    SUBKV(FA, FB, 1, 2, 2, 1, NOP_);
    WAIT_BAR(0);
    hb1 = __hip_atomic_load(HEARTBEAT, RLX_AGENT);
    SUBKV(FB, FA, 1, 2, 0, 0, TOGV());
    SUBKV(FA, FB, 1, 2, 0, 1, NOP_);
    SUBKV(FB, FA, 0, 0, 1, 1, NOP_);
    QTAIL(HA);
    HANDSHAKE();
    asm volatile("s_barrier" ::: "memory");
    const bf16x8 ones = asbf((u32x4){0x3f803f80u, 0x3f803f80u, 0x3f803f80u, 0x3f803f80u});
    f32x4 kv[2][2] = {}, z[2] = {};
#pragma unroll
    for (int tb = 0; tb < 2; ++tb) {
      const bf16x8 v0 = asbf(pack44(acc[2 * tb][4], acc[2 * tb + 1][4])), v1 = asbf(pack44(acc[2 * tb][5], acc[2 * tb + 1][5]));
#pragma unroll
      for (int dt = 0; dt < 2; ++dt) {
        const bf16x8 pk = asbf((u32x4){pkh[4 * tb + 2 * dt][0], pkh[4 * tb + 2 * dt][1], pkh[4 * tb + 2 * dt + 1][0], pkh[4 * tb + 2 * dt + 1][1]});
        kv[dt][0] = mfma16(pk, v0, kv[dt][0]); kv[dt][1] = mfma16(pk, v1, kv[dt][1]); z[dt] = mfma16(pk, ones, z[dt]);
      }
    }
    SBAR();
    pq[0] = asbf(pack44(phi4(acc[0][0]), phi4(acc[0][1]))); PIN(pq[0]);
    SBAR();
#pragma unroll
    for (int et = 0; et < 2; ++et) { *(LAS f32x4*)(P + wid * 4096 + et * 2048 + lane * 32) = kv[0][et]; *(LAS f32x4*)(P + wid * 4096 + et * 2048 + lane * 32 + 16) = kv[1][et]; }
    if (c16 == 0) { *(LAS f32x4*)(P + SLOT + wid * 128 + g4 * 16) = z[0]; *(LAS f32x4*)(P + SLOT + wid * 128 + 64 + g4 * 16) = z[1]; }
    unsigned pk = __hip_atomic_load(FLAG(1, tm, tn, 1), RLX_AGENT);
    SBAR();
    pq[1] = asbf(pack44(phi4(acc[1][0]), phi4(acc[1][1]))); PIN(pq[1]);
    PIN(pk);
    if (wid == 0) { unsigned spins = 0;
      SPIN while (!__all(pk != 0u)) { if (spin_step(spins, fl, true)) break; pk = poll32(FLAG(1, tm, tn, 1)); } }
    asm volatile("s_waitcnt lgkmcnt(0)\n\ts_barrier" ::: "memory");
  }
#undef HA
#undef HANDSHAKE
#undef HK
#undef HV
#undef QTAIL
#undef SUB2
#undef SUBKV
#undef STAGE
#undef SUBA
#undef SUBB
#undef CATW
#undef MF
#undef XP
#undef WP
#undef RX
#undef RA
#undef RB
#undef TOGV
#undef BEAT

  const int qg = wm, kh = wn, hd = tn * 2 + (BRB ? 0 : 1);
  if (BRB && tid == 0) __hip_atomic_store((gu64*)FLAG(1, tm, tn, 0), 0x100000001ull, RLX_AGENT);
  const unsigned vl = lane * 16;
  const unsigned gkv = WS_KV + (unsigned)hd * KV_H + wid * 4096u, dkv = lds0 + wid * 4096;
  unsigned rdy = 1u << tm;
  gu32* const flk = FLAG(1, lane & 7, tn, BRB ? 0 : 1);
#define CH(j) ((j) < 4 ? (tm & 4) | ((tm + (j)) & 3) : (~tm & 4) | ((tm + (j)) & 3))
#define PEEK_KV() peek_a(flk)
#define PEEK_IF() do { if (__builtin_expect(rdy != ~0u, 0)) peek_a(flk); } while (0)
#define NEED_KV(j) do { if (__builtin_expect(rdy != ~0u, 0)) { rdy |= (unsigned)seen_a(); unsigned sp_ = 0; \
      SPIN while (!((rdy >> CH(j)) & 1u)) { rdy |= (unsigned)__ballot(poll32(flk) != 0u); if (spin_step(sp_, fl, true)) break; } } } while (0)
  unsigned gch;
#define SRC(j) do { gch = gkv + (unsigned)CH(j) * 32768u; asm volatile("" : "+s"(gch)); } while (0)
#define DKV(slot, p) dma16<true>(sws, vl, gch, (p) * 1024, dkv, (slot) * SLOT + (p) * 1024)
  const unsigned rro = kh * 16384 + qg * 1024 - wid * 4096;
#define DKR(slot, r) dma16<true>(sws, vl, gch + rro, (r) * 4096, dkv + rro, (slot) * SLOT + (r) * 4096)
  const int tm3 = (xcd & 3) * 4 + (bk & 3), tn3 = (xcd >> 2) * 4 + (bk >> 2);
  const srd_t swo = mksrd(wo, 2097152u);
  const unsigned go3 = WS_O + (unsigned)((BRB ? 8 : 0) + (tm3 >> 1)) * O_TM + (unsigned)((tm3 & 1) * 8 + wid) * 32768u, gw3 = (unsigned)((wid >> 2) * 64 + (wid & 3) * 8) * 2048u + (unsigned)tn3 * 256u;
  const unsigned d3 = lds0 + wid * 2048;
#define H0(s) ((s) < 8 ? 4 * (s) : 4 * ((s) - 8) + 1)
#define OH(s) (BRB ? ((s) % 5 == 0 ? 4 : 5 + (s) % 5) : ((s) % 5 < 2 ? (s) % 5 : (s) % 5 == 2 ? 5 : (s) % 5 == 3 ? 4 : 6))
#define WH(s) ((s) < 3 ? (s) : BRB ? (((s) - 3) % 5 == 4 ? 5 : ((s) - 3) % 5) : (((s) - 3) % 5 < 2 ? 2 + ((s) - 3) % 5 : 5 + ((s) - 3) % 5))
#define DO3(s, p) dma16<true>(sws, vl, go3, H0(s) * 1024 + (p) * 2048, d3, OH(s) * 16384 + (p) * 1024)
#define DW3(s, p) dma16<false>(swo, (p) ? vw1 : vw0, gw3, H0(s) * 65536 + (p) * 8192, d3, WH(s) * 16384 + (p) * 1024)
#define FLO(par) FLAG(BRB && !(par), tm3 >> 1, lane & 15, BRB ? 2 : 1)
#define NEED_O(par) do { unsigned sp_ = 0; SPIN while (!__all(poll32(FLO(par)) != 0u)) if (spin_step(sp_, fl, true)) break; } while (0)

  bf16x8 qa0, qa1, qb0, qb1;
  if (BRB) {
    const lds_cp p = L + qg * 4096 + vl;
    qa0 = ldsr128(p + kh * 2048); qa1 = ldsr128(p + kh * 2048 + 1024); qb0 = ldsr128(p + (kh ^ 1) * 2048); qb1 = ldsr128(p + (kh ^ 1) * 2048 + 1024);
  } else {
    SRC(0); DKR(3, 0); DKR(3, 1);
    const unsigned gq = WS_Q + (unsigned)(hd * 64 + tm * 8 + qg * 2) * 2048u;
    qa0 = asbf(ld_dev(sws, vl, gq + kh * 2048)); qa1 = asbf(ld_dev(sws, vl + 1024, gq + kh * 2048)); qb0 = asbf(ld_dev(sws, vl, gq + (kh ^ 1) * 2048)); qb1 = asbf(ld_dev(sws, vl + 1024, gq + (kh ^ 1) * 2048));
    SBAR();
    const lds_cp p = L + kh * 16384 + (tid & 255) * 16, pz = L + SLOT + kh * 512 + (lane & 31) * 4;
    f32x4 a = (*(LAS const f32x4*)p + *(LAS const f32x4*)(p + 4096)) + (*(LAS const f32x4*)(p + 8192) + *(LAS const f32x4*)(p + 12288));
    float zz = (*(LAS const float*)pz + *(LAS const float*)(pz + 128)) + (*(LAS const float*)(pz + 256) + *(LAS const float*)(pz + 384));
    PIN(a); PIN(zz);
#pragma unroll
    for (int a2 = 2; a2 < 4; ++a2) { pq[a2] = asbf(pack44(phi4(acc[a2][0]), phi4(acc[a2][1]))); PIN(pq[a2]); }
    P2_CONST();
    SBAR();
    asm volatile("s_waitcnt vmcnt(0)\n\ts_barrier" : "+v"(qa0), "+v"(qa1), "+v"(qb0), "+v"(qb1) :: "memory");
    DKR(3, 2); DKR(3, 3);
    st_dev(sws, (tid & 255) * 16, WS_KVP + (unsigned)((tn * 2 + kh) * 8 + tm) * 4096u, __builtin_bit_cast(u32x4, a));
    __builtin_amdgcn_raw_buffer_store_b32(__float_as_uint(zz), sws, (qg == 0 && lane < 32) ? WS_ZP + (((tn * 2 + kh) * 32 + lane) * 8 + tm) * 4 : OOB, 0, 16);
  }
  f32x16 nma, nmb;
  bf16x8 k0 = ldsr128(c3), k1 = ldsr128(c3 + 1024), v0, v1;
  { const bf16x8 r0 = ldsr128(L + 3 * SLOT + vl), r1 = ldsr128(L + 3 * SLOT + vl + 1024);
    f32x16 s = mfma32(r0, qa0, (f32x16){}), t = mfma32(r0, qb0, (f32x16){}); s = mfma32(r1, qa1, s); t = mfma32(r1, qb1, t);
    nma = splat16(-max16(s)); nmb = splat16(-max16(t)); }
  PIN(nma); PIN(nmb);
  f32x16 SA = mfma32(k0, qa0, nma), TA = mfma32(k0, qb0, nmb), SB, TB;
  SA = mfma32(k1, qa1, SA); TA = mfma32(k1, qb1, TA);
#define EX(S, j) do { float e_ = __builtin_amdgcn_exp2f(S[j]); PIN(e_); S[j] = e_; } while (0)
#define E2(S, j) EX(S, j); EX(S, j + 1); SBAR()
#define E3(S, j) EX(S, j); EX(S, j + 1); EX(S, j + 2); SBAR()
#define STEPF(FIRST, PR, C, D, P, Q, pv, pk, G0, G1, G2, G3, H0, H1, H2, H3) do { \
    if (!FIRST) Oa = smf32(v1, hi8(P), Oa, idx);  SBAR(); PRIO(PR); k0 = ldsr128(pk); SBAR(); G0; SBAR(); E3(C, 0); \
    if (!FIRST) La = smf16(sel, hi8(P), La, idx); SBAR(); v0 = ldsr128((pv) + 2048); SBAR(); G1; SBAR(); E2(C, 3); \
    if (!FIRST) Ob = smf32(v1, hi8(Q), Ob, idx);  SBAR(); k1 = ldsr128((pk) + 1024); SBAR(); G2; SBAR(); E3(C, 5); \
    if (!FIRST) Lb = smf16(sel, hi8(Q), Lb, idx); SBAR(); v1 = ldsr128((pv) + 3072); SBAR(); G3; SBAR(); PIN(k0); SBAR(); E2(D, 0); \
    P = mfma32(k0, qa0, nma);         SBAR(); PIN(v0); PIN(Oa); SBAR(); E3(D, 2); \
    Oa = smf32(v0, lo8(C), Oa, idx);  SBAR(); PIN(k1); PIN(La); SBAR(); E3(D, 5); \
    P = mfma32(k1, qa1, P);           SBAR(); H0; SBAR(); E3(C, 8); \
    La = smf16(sel, lo8(C), La, idx); SBAR(); PIN(Ob); PIN(Lb); SBAR(); E2(C, 11); \
    Q = mfma32(k0, qb0, nmb);         SBAR(); H1; SBAR(); E3(C, 13); \
    Ob = smf32(v0, lo8(D), Ob, idx);  SBAR(); H2; SBAR(); E3(D, 8); \
    Q = mfma32(k1, qb1, Q);           SBAR(); H3; SBAR(); E3(D, 11); \
    Lb = smf16(sel, lo8(D), Lb, idx); SBAR(); PIN(v1); SBAR(); E2(D, 14); } while (0)
#define STEP(...) STEPF(false, __VA_ARGS__)
#define STEP1(...) STEPF(true, __VA_ARGS__)
#define N4 NOP_, NOP_, NOP_, NOP_
#define S0(c, ...) STEP(2, SA, TA, SB, TB, c, c + 4096, __VA_ARGS__)
#define S1(c, ...) STEP(1, SB, TB, SA, TA, c + 4096, c + 8192, __VA_ARGS__)
#define S2(c, ...) STEP(0, SA, TA, SB, TB, c + 8192, c + 12288, __VA_ARGS__)
#define S3(c, n, ...) STEP(3, SB, TB, SA, TA, c + 12288, n, __VA_ARGS__)
#define D4(slot) DKV(slot, 0), DKV(slot, 1), DKV(slot, 2), DKV(slot, 3)
#define IFA(x) do { if (!BRB) { x; } } while (0)
  gu32* const flp = FLAG(0, lane & 7, tn, 0);
  const unsigned gp = WS_KVP + (unsigned)tn * 65536u + wid * 8192u, dp = lds0 + wid * 8192;
#define DP(p) IFA(dma16<true>(sws, vl, gp, (p) * 1024, dp, (p) * 1024))
  STEP1(3, SA, TA, SB, TB, c3, c3 + 4096, N4, NOP_, NOP_, NOP_, PEEK_KV());
  IFA(WAIT_BAR(3));
  S1(c3, N4, SRC(1), NOP_, NOP_, NOP_);
  DRAIN(); NEED_KV(1);
  S2(c3, D4(4), PEEK_KV(), SRC(2), NOP_, NOP_);
  WAIT_BAR(0);
  NEED_KV(2);
  if (!BRB && tid == 0) __hip_atomic_store(FLAG(0, tm, tn, 0), 1u, RLX_AGENT);
#define TOUCH_M() touch(swo, lane < 16 ? ((tm * 16 + tn) * 128 + wid * 16 + lane) * 128 : OOB, 0)
  S3(c3, c4, TOUCH_M(), NOP_, NOP_, NOP_, D4(2));
  S0(c4, N4, NOP_, PEEK_IF(), IFA(peek_b(flp)), SRC(3));
  S1(c4, N4, N4);
  S2(c4, N4, N4);
  WAIT_BAR(0);
  NEED_KV(3);
  if (!BRB && __builtin_expect(~seen_b() != 0ull, 0)) { unsigned sp = 0; SPIN while (!__all(poll32(flp) != 0u)) if (spin_step(sp, fl, true)) break; }
  S3(c4, c2, D4(3), DP(0), DP(1), DP(2), DP(3));
  u32x4 zp;
  S0(c2, PEEK_IF(), SRC(4), IFA(zp = ld_dev(sws, vl, WS_ZP + (unsigned)(tn * 2 + kh) * 1024u)), NOP_, DP(4), DP(5), DP(6), DP(7));
  S1(c2, N4, N4);
  S2(c2, N4, N4);
  WAIT_BAR(0);
  NEED_KV(4);
  float zs; f32x4 u0, u1, u2, u3, ua, ub;
  lds_p pr = (lds_p)lds + kh * SLOT + (tid & 255) * 16;
#define RU(u, m) u = *(LAS const f32x4*)(pr + (m) * 4096)
  IFA(PIN(zp); PIN(pr));
  S3(c2, c3, IFA(RU(u0, 0); RU(u1, 1)), IFA(RU(u2, 2); RU(u3, 3)),
             IFA(zs = __uint_as_float(zp[0]) + __uint_as_float(zp[1]); PIN(zs); zs += __uint_as_float(zp[2]); zs += __uint_as_float(zp[3]); PIN(zs)),
             IFA(ua = add4s(u0, u1); RU(u0, 4); RU(u1, 5)),
             { DKV(4, 0); IFA(ub = add4s(u2, u3); RU(u2, 6); RU(u3, 7)); }, { DKV(4, 1); IFA(ua = add4s(ua, ub)); },
             { DKV(4, 2); IFA(ub = add4s(u0, u1)); }, { DKV(4, 3); IFA(u2 = add4s(u2, u3)); });
  S0(c3, IFA(ub = add4s(ub, u2)), IFA(ua = add4s(ua, ub); *(LAS u32x2*)pr = ((u32x2){cvtpk(ua[0], ua[1]), cvtpk(ua[2], ua[3])})), PEEK_IF(), SRC(5),
         IFA(zs += __uint_as_float(__builtin_amdgcn_update_dpp(0, __float_as_uint(zs), 0xB1, 0xF, 0xF, true)); PIN(zs)), NOP_, NOP_, NOP_);
  float zd[8]; bf16x8 zf, kf0, kf1;
#define ZD(e) zd[e] = __uint_as_float(__builtin_amdgcn_ds_bpermute(32 * g4 + 128 * ((e) >> 2) + 8 * ((e) & 3), __float_as_uint(zs)))
  S1(c3, N4, IFA(ZD(0); ZD(1)), IFA(ZD(2); ZD(3)), IFA(ZD(4); ZD(5)), IFA(ZD(6); ZD(7)));
  S2(c3, N4, IFA(zf = asbf(((u32x4){cvtpk(zd[0], zd[1]), cvtpk(zd[2], zd[3]), cvtpk(zd[4], zd[5]), cvtpk(zd[6], zd[7])})); PIN(zf)), NOP_, NOP_, NOP_);
  WAIT_BAR(0);
  NEED_KV(5);
#define KF(T) ({ const lds_cp p_ = L + kh * SLOT + (c16 >> 3) * 2048 + (g4 * 16 + 8 * (T) + (c16 & 7)) * 32; \
      const u32x2 lo_ = *(LAS const u32x2*)p_, hi_ = *(LAS const u32x2*)(p_ + 16); asbf((u32x4){lo_[0], lo_[1], hi_[0], hi_[1]}); })
  f32x4 dn[4], n0[4], n1[4]; float rd; u32x2 oh;
  const unsigned ol = vl + WS_O + (unsigned)tm * O_TM + (unsigned)(qg * 128 + tn * 2 + kh) * 1024u;
#define LD(a) IFA(dn[a] = mfma16(zf, pq[a], (f32x4){}))
#define L0(a) IFA(n0[a] = mfma16(kf0, pq[a], (f32x4){}))
#define L1(a) IFA(n1[a] = mfma16(kf1, pq[a], (f32x4){}))
#define LA(a) IFA(rd = __builtin_amdgcn_rcpf(dn[a][0] + 1e-6f); const f32x4 t_ = mul4s(n0[a], rd); oh = ((u32x2){cvtpk(t_[0], t_[1]), cvtpk(t_[2], t_[3])}); PIN(oh))
#define LB(a) IFA(const f32x4 t_ = mul4s(n1[a], rd); st_dev(sws, ol, (a) * 32768, ((u32x4){oh[0], oh[1], cvtpk(t_[0], t_[1]), cvtpk(t_[2], t_[3])})))
  S3(c3, c4, IFA(kf0 = KF(0)), IFA(kf1 = KF(1)), NOP_, LD(0), { DKV(2, 0); L0(0); }, { DKV(2, 1); L1(0); }, { DKV(2, 2); LD(1); }, { DKV(2, 3); L0(1); });
  S0(c4, L1(1), LA(0), LB(0), LD(2), L0(2), L1(2), LA(1), LB(1));
  S1(c4, LD(3), L0(3), L1(3), LA(2), LB(2), LA(3), LB(3), { PEEK_IF(); SRC(6); });
  S2(c4, N4, N4);
  WAIT_BAR(0);
  NEED_KV(6);
  if (!BRB && tid == 0) __hip_atomic_store(FLAG(0, tm, tn, 1), 1u, RLX_AGENT);
  int ln3 = lane; PIN(ln3);
  const unsigned vw0 = (ln3 >> 4) * 2048 + (((ln3 & 15) ^ ((ln3 >> 4) << 1)) << 4), vw1 = vw0 ^ 128;
  S3(c4, c2, DW3(0, 0), DW3(0, 1), DW3(1, 0), DW3(1, 1), D4(3));
  S0(c2, PEEK_IF(), SRC(7), NOP_, NOP_, DW3(2, 0), DW3(2, 1), DW3(BRB ? 6 : 4, 0), DW3(BRB ? 6 : 4, 1));
  S1(c2, N4, N4);
  const int pn = (wid >> 1) & 1, g3 = ln3 >> 4, q3 = (ln3 & 15) >> 2, p3 = ln3 & 3;
  const unsigned aw3 = (kh * 32 + 16 * (g3 >> 1) + 4 * (g3 & 1) + q3) * 256 + (((pn * 8 + (p3 >> 1)) ^ ((4 * (g3 & 1) + q3) << 1)) << 4) + (p3 & 1) * 8;
  lds_cp bw[4][3];
  S2(c2, N4, { bw[0][0] = L + aw3; PIN(bw[0][0]); }, { bw[1][0] = L + (aw3 ^ 32); PIN(bw[1][0]); }, { bw[2][0] = L + (aw3 ^ 64); PIN(bw[2][0]); }, { bw[3][0] = L + (aw3 ^ 96); PIN(bw[3][0]); });
  WAIT_BAR(0);
  NEED_KV(7);
  bf16x8 wr[3][4];
#define WRG(s, T) wr[s][T] = cat8(ldstr(bw[T][0] + (s) * 16384), ldstr(bw[T][0] + (s) * 16384 + 2048))
  S3(c2, c3, WRG(0, 0), WRG(0, 1), WRG(0, 2), WRG(0, 3), D4(4));
  S0(c3, WRG(1, 0), WRG(1, 1), WRG(1, 2), WRG(1, 3), WRG(2, 0), WRG(2, 1), WRG(2, 2), WRG(2, 3));
  S1(c3, IFA(peek_b(FLO(0))), NOP_, NOP_, NOP_, N4);
  S2(c3, N4, N4);
  WAIT_BAR(0);
  asm volatile("" : "+v"(wr[0][0]), "+v"(wr[0][1]), "+v"(wr[0][2]), "+v"(wr[0][3]), "+v"(wr[1][0]), "+v"(wr[1][1]), "+v"(wr[1][2]), "+v"(wr[1][3]),
                    "+v"(wr[2][0]), "+v"(wr[2][1]), "+v"(wr[2][2]), "+v"(wr[2][3]));
  if (!BRB && __builtin_expect(~seen_b() != 0ull, 0)) NEED_O(0);
#define PRE(i, p) do { if (BRB) DW3((i) < 3 ? 3 + (i) : 7, p); else if ((i) == 2) DW3(3, p); else DO3((i) < 2 ? (i) : 2, p); } while (0)
  S3(c3, c4, N4, PRE(0, 0), PRE(0, 1), PRE(1, 0), PRE(1, 1));
  S0(c4, N4, PRE(2, 0), PRE(2, 1), PRE(3, 0), PRE(3, 1));
  S1(c4, N4, N4);
  S2(c4, N4, N4);
  asm volatile("s_barrier" ::: "memory");
  { lds_p d = (lds_p)lds + 3 * SLOT + wid * 4096 + vl;
#define WO(c) *(LAS f32x4*)(d + (c) * 1024) = (f32x4){Ob[4 * (c)], Ob[4 * (c) + 1], Ob[4 * (c) + 2], Ob[4 * (c) + 3]}; SBAR()
    Ob = smf32(v1, hi8(TA), Ob, idx);  SBAR(); v0 = ldsr128(c4 + 12288 + 2048); SBAR(); E3(TB, 0);
    Lb = smf16(sel, hi8(TA), Lb, idx); SBAR(); E2(TB, 3);
    Oa = smf32(v1, hi8(SA), Oa, idx);  SBAR(); v1 = ldsr128(c4 + 12288 + 3072); SBAR(); E3(TB, 5);
    La = smf16(sel, hi8(SA), La, idx); SBAR(); PIN(v0); SBAR(); E2(TB, 8);
    Ob = smf32(v0, lo8(TB), Ob, idx);  SBAR(); E3(TB, 10);
    Lb = smf16(sel, lo8(TB), Lb, idx); SBAR(); PIN(v1); SBAR(); E3(TB, 13); E2(SB, 0);
    Ob = smf32(v1, hi8(TB), Ob, idx);  SBAR(); E3(SB, 2);
    Lb = smf16(sel, hi8(TB), Lb, idx); SBAR(); E3(SB, 5); E2(SB, 8);
    Oa = smf32(v0, lo8(SB), Oa, idx);  SBAR(); E3(SB, 10); WO(0); WO(1);
    La = smf16(sel, lo8(SB), La, idx); SBAR(); E3(SB, 13); WO(2); WO(3);
    Oa = smf32(v1, hi8(SB), Oa, idx);  SBAR(); *(LAS float*)((lds_p)lds + 2 * SLOT + wid * 256 + lane * 4) = Lb[0]; SBAR();
    La = smf16(sel, hi8(SB), La, idx); SBAR();
#undef WO
  }
  PRIO(0);
  const int pm = wid & 1, c3_ = ln3 & 15;
  lds_cp bo[2][3];
#pragma unroll
  for (int u = 0; u < 3; ++u) { const unsigned ao = pm * 8192 + kh * 1024 + vl + u * 65536;
    bo[0][u] = L + (ao + kh * 4096); bo[1][u] = L + (ao + (kh ^ 1) * 4096); PIN(bo[0][u]); PIN(bo[1][u]);
#pragma unroll
    for (int T = 0; T < 4; ++T) if (u) { bw[T][u] = bw[T][0] + u * 65536; PIN(bw[T][u]); } }
  struct F3 { bf16x8 o[4], w[4]; } GA, GB;
#define RO0() do { _Pragma("unroll") for (int a = 0; a < 4; ++a) GA.o[a] = ldsr128(bo[a >> 1][OH(0) >> 2] + (OH(0) & 3) * 16384 + (a & 1) * 2048); } while (0)
  WAIT_BAR(0);
  float l = La[0];
  { const lds_cp s = L + 3 * SLOT + (wid ^ 4) * 4096 + vl;
    l += *(LAS const float*)(L + 2 * SLOT + (wid ^ 4) * 256 + lane * 4);
#pragma unroll
    for (int c = 0; c < 4; ++c) { const f32x4 u = *(LAS const f32x4*)(s + c * 1024);
#pragma unroll
      for (int e = 0; e < 4; ++e) Oa[4 * c + e] += u[e]; } }
  IFA(RO0());
  float am = __builtin_elementwise_maximum(__builtin_fabsf(Oa[0]), __builtin_fabsf(Oa[1]));
#pragma unroll
  for (int j = 2; j < 16; j += 2) am = __builtin_elementwise_maximum(__builtin_elementwise_maximum(am, __builtin_fabsf(Oa[j])), __builtin_fabsf(Oa[j + 1]));
  if (__builtin_expect(!__all(l < 0x1p60f && am < 0x1p120f), 0)) {
    float m = -__builtin_inff(); Oa = (f32x16){}; f32x4 Ls = {};
    SPIN for (int t = 0; t < 64; ++t) {
      const unsigned g = WS_KV + (unsigned)hd * KV_H + t * 4096u;
      k0 = asbf(ld_dev(sws, vl, g)); k1 = asbf(ld_dev(sws, vl, g + 1024)); v0 = asbf(ld_dev(sws, vl, g + 2048)); v1 = asbf(ld_dev(sws, vl, g + 3072));
      f32x16 s = mfma32(k0, qa0, (f32x16){}); s = mfma32(k1, qa1, s);
      const float mn = __builtin_fmaxf(m, max16(s)), al = __builtin_amdgcn_exp2f(m - mn);
      m = mn; Oa *= al; Ls *= al;
#pragma unroll
      for (int j = 0; j < 16; ++j) s[j] = __builtin_amdgcn_exp2f(s[j] - mn);
      Oa = smf32(v0, lo8(s), Oa, idx); Oa = smf32(v1, hi8(s), Oa, idx);
      Ls = smf16(sel, lo8(s), Ls, idx); Ls = smf16(sel, hi8(s), Ls, idx);
    }
    l = Ls[0];
  }
  Oa *= __builtin_amdgcn_rcpf(l);
  { const unsigned o = ((ln3 >> 4) & 1) * 32768 + ((ln3 & 15) + 16 * (ln3 >> 5)) * 16, g = WS_O + (unsigned)(8 + tm) * O_TM + (unsigned)((qg * 2 + kh) * 64 + hd) * 1024u;
    st_dev(sws, o, g, pack8(Oa, 0)); st_dev(sws, o, g + 512, pack8(Oa, 1)); }
  gu32* const flob = FLAG(BRB, tm, tn, 2);

  {
    if (BRB) {
      WAIT_BAR(0);
      if (tid == 0) __hip_atomic_store(flob, 1u, RLX_AGENT);
      if (wid == 1) NEED_O(0);
      asm volatile("s_barrier" ::: "memory");
      DO3(0, 0); DO3(0, 1); DO3(1, 0); DO3(1, 1); DO3(2, 0); DO3(2, 1); DO3(3, 0); DO3(3, 1); DO3(4, 0); DO3(4, 1);
      WAIT_BAR(8);
      RO0();
    }
    f32x4 ac[4][4];
#pragma unroll
    for (int T = 0; T < 4; ++T)
#pragma unroll
      for (int a = 0; a < 4; ++a) ac[T][a] = (f32x4){};
    s16x4 ga[4], gb[4];
#define MG(F, T, a) ac[T][a] = mfma16(F.w[T], F.o[a], ac[T][a]); SBAR()
#define RO(R, N, n, a) if (R) N.o[a] = ldsr128(bo[(a) >> 1][OH(n) >> 2] + (OH(n) & 3) * 16384 + ((a) & 1) * 2048); SBAR()
#define RWA(R, n, T) if (R) ga[T] = ldstr(bw[T][WH(n) >> 2] + (WH(n) & 3) * 16384); SBAR()
#define RWB(R, n, T) if (R) gb[T] = ldstr(bw[T][WH(n) >> 2] + (WH(n) & 3) * 16384 + 2048); SBAR()
#define D3(t, k) do { constexpr int i_ = p3_cum(BRB, t) + (k); if (i_ < p3_cum(BRB, (t) + 1)) { constexpr int s_ = p3_stage(BRB, i_); \
      if (p3_isw(BRB, i_)) DW3(s_, i_ & 1); else DO3(s_, i_ & 1); } SBAR(); } while (0)
#define STG(t, F, N, HOOK) do { constexpr int n_ = (t) + 1; constexpr bool R = true, RW = n_ >= 3, RW2 = RW && n_ < 13; \
      asm volatile("s_waitcnt vmcnt(%0) lgkmcnt(0)\n\ts_barrier" :: "i"(p3_cum(BRB, t) - p3_need(BRB, n_) + (!BRB && (t) == 0 ? 2 : 0)) : "memory"); HOOK; SBAR(); \
      MG(F,0,0); RO(R,N,n_,0);  MG(F,0,1); RWA(RW,n_,0); MG(F,0,2); RWB(RW,n_,0); MG(F,0,3); D3(t, 0); \
      MG(F,1,0); RO(R,N,n_,1);  MG(F,1,1); RO(R,N,n_,2); MG(F,1,2); RO(R,N,n_,3); MG(F,1,3); D3(t, 1); \
      MG(F,2,0); RWA(RW,n_,1); RWB(RW,n_,1); MG(F,2,1); RWA(RW2,n_,2); RWB(RW2,n_,2); MG(F,2,2); RWA(RW2,n_,3); RWB(RW2,n_,3); MG(F,2,3); D3(t, 2); \
      MG(F,3,0); MG(F,3,1); MG(F,3,2); D3(t, 3); MG(F,3,3); \
      _Pragma("unroll") for (int c_ = 0; c_ < (n_ < 13 ? 4 : 2); ++c_) N.w[c_] = RW ? cat8(ga[c_], gb[c_]) : wr[RW ? 0 : n_][c_]; } while (0)
#pragma unroll
    for (int T = 0; T < 4; ++T) GA.w[T] = wr[0][T];
    STG(0, GA, GB, NOP_);
    STG(1, GB, GA, if (!BRB && tid == 0) __hip_atomic_store(flob, 1u, RLX_AGENT));
    STG(2, GA, GB, NOP_);
    STG(3, GB, GA, if (BRB && wid == 0) peek_b(FLO(1)));   STG(4, GA, GB, NOP_);
    if (BRB && wid == 0) { asm volatile("s_waitcnt vmcnt(4)" ::: "memory"); if (__builtin_expect(~seen_b() != 0ull, 0)) NEED_O(1); }
    STG(5, GB, GA, NOP_);   STG(6, GA, GB, NOP_);   STG(7, GB, GA, NOP_);
    STG(8, GA, GB, NOP_);   STG(9, GB, GA, NOP_);   STG(10, GA, GB, NOP_);  STG(11, GB, GA, NOP_);
    STG(12, GA, GB, NOP_);
    const srd_t sout = mksrd(out, 8388608u);
    const unsigned od = (tm3 * 128 + pm * 64 + kh * 32 + c3_) * 4096 + (BRB ? 2048 : 0) + tn3 * 256 + pn * 128 + (g3 & 1) * 32 + (g3 >> 1) * 16;
    lds_p d1 = (lds_p)lds + (BRB ? 6 : 8) * 16384 + wid * 4096 + vl, d2 = (lds_p)lds + (kh ? 5 : BRB ? 3 : 1) * 16384 + (wid & 3) * 4096 + vl;
    lds_cp s1 = L + (BRB ? 6 : 8) * 16384 + (wid ^ 4) * 4096 + vl, s2 = L + (kh ? (BRB ? 3 : 1) : 5) * 16384 + (wid & 3) * 4096 + vl;
    PIN(d1); PIN(d2); PIN(s1); PIN(s2);
    f32x4 pu[2][2];
#define HS(F, T, N, R, n, Tn, K0, K1, K2, K3) do { \
      MG(F,T,2); RO(R,N,n,0); MG(F,T,3); RWA(R,n,Tn); MG(F,T+1,2); RWB(R,n,Tn); K0; SBAR(); MG(F,T+1,3); RO(R,N,n,1); RO(R,N,n,2); K1; SBAR(); \
      MG(F,T,0); RO(R,N,n,3); MG(F,T,1); RWA(R,n,Tn+1); K2; SBAR(); MG(F,T+1,0); RWB(R,n,Tn+1); MG(F,T+1,1); K3; SBAR(); \
      if (R) { N.w[Tn] = cat8(ga[Tn], gb[Tn]); N.w[Tn+1] = cat8(ga[Tn+1], gb[Tn+1]); } } while (0)
#define WR(d, T, a) *(LAS f32x4*)(d + ((T) & 1) * 2048 + ((a) - 2) * 1024) = ac[T][a]
#define RP(sp, a) do { pu[a][0] = *(LAS const f32x4*)(sp + (a) * 1024); pu[a][1] = *(LAS const f32x4*)(sp + 2048 + (a) * 1024); } while (0)
#define FIN(T, a) do { const f32x4 u_ = add4s(ac[T][a], pu[a][0]), v_ = add4s(ac[T + 1][a], pu[a][1]); \
      const auto r0 = __builtin_amdgcn_permlane16_swap(cvtpk(u_[0], u_[1]), cvtpk(v_[0], v_[1]), false, false); \
      const auto r1 = __builtin_amdgcn_permlane16_swap(cvtpk(u_[2], u_[3]), cvtpk(v_[2], v_[3]), false, false); \
      __builtin_amdgcn_raw_buffer_store_b128((u32x4){r0[0], r1[0], r0[1], r1[1]}, sout, od + (a) * 65536 + (T) * 32, 0, 16); } while (0)
    WAIT_BAR(4);
    HS(GB, 0, GA, 1, 14, 0, NOP_, NOP_, NOP_, NOP_);
    WAIT_BAR(0);
    HS(GA, 0, GB, 1, 15, 0, NOP_, NOP_, NOP_, NOP_);
    HS(GB, 0, GA, 1, 13, 2, NOP_, NOP_, NOP_, NOP_);
    HS(GA, 2, GB, 1, 14, 2, WR(d1, 0, 2), WR(d1, 0, 3), WR(d1, 1, 2), WR(d1, 1, 3));
    asm volatile("s_waitcnt lgkmcnt(0)\n\ts_barrier" ::: "memory");
    RP(s1, 0); RP(s1, 1); SBAR();
    HS(GB, 2, GA, 1, 15, 2, NOP_, FIN(0, 0), NOP_, FIN(0, 1));
    HS(GA, 2, GB, 0, 15, 2, NOP_, NOP_, { WR(d2, 2, 2); WR(d2, 2, 3); }, { WR(d2, 3, 2); WR(d2, 3, 3); });
    asm volatile("s_waitcnt lgkmcnt(0)\n\ts_barrier" ::: "memory");
    RP(s2, 0); RP(s2, 1);
    FIN(2, 0); FIN(2, 1);
  }
}

typedef unsigned u32x16 __attribute__((ext_vector_type(16)));
__global__ __launch_bounds__(512, 2) __attribute__((amdgpu_num_vgpr(252)))
void k_fused(const char* xA, const char* wA, const char* woA, const char* xB, const char* wB, const char* woB, char* ws_, char* out_) {
  extern __shared__ __attribute__((aligned(16))) char lds[];
  Entry e;
  u32x16 ka;
  asm volatile("s_load_dwordx16 %0, %1, 0x0" : "=s"(ka) : "s"(__builtin_amdgcn_kernarg_segment_ptr()));
  const int xcd = blockIdx.x & 7, bk = blockIdx.x >> 4, isB = (blockIdx.x >> 3) & 1;
  const int lane = threadIdx.x & 63, wid = __builtin_amdgcn_readfirstlane(threadIdx.x >> 6);
  const int tm = (xcd & 1) * 4 + (bk & 3), tn = (xcd >> 1) * 4 + (bk >> 2);
  const unsigned lds0 = (unsigned)(uintptr_t)lds;
  const unsigned vx0 = (lane >> 3) * 2048 + (((lane & 7) ^ (lane >> 4)) << 4), vx1 = vx0 ^ 64;
  const unsigned vw = (lane >> 3) * 6144 + (((lane & 7) ^ (((lane >> 4) & 1) << 2) ^ ((wid & 1) << 1)) << 4);
  const unsigned gx0 = (unsigned)(tm * 256 + wid * 32) * 2048u, gw0 = (unsigned)(wid * 8) * 6144u + (unsigned)tn * 128u;
  unsigned k3 = (4 * (xcd >> 1) + 8 * (xcd & 1) + isB) & 15, gx, gw = 0, gv;
  const unsigned dx = lds0 + L1_X + wid * 4096, dqk = lds0 + L1_WQK + wid * 1024, dv = lds0 + L1_WV + wid * 1024;
  SETK();
  asm volatile("s_waitcnt lgkmcnt(0)" : "+s"(ka), "+s"(gx), "+s"(gw) : "v"(vx0), "v"(vx1), "v"(vw));
#define KARG(i) (char*)(((unsigned long long)ka[2 * (i) + 1] << 32) | ka[2 * (i)])
  const srd_t sx = mksrd(isB ? KARG(3) : KARG(0), 2048u * 1024u * 2u), sw = mksrd(isB ? KARG(4) : KARG(1), 1024u * 3072u * 2u);
  DMA_WQK(0, 0); DMA_X(0, 0); DMA_X(0, 1); DMA_X(0, 2); DMA_X(0, 3); DMA_WQK(0, 1); DMA_WV(gw, 0);
  e.ws = KARG(6); e.out = KARG(7); e.wo = isB ? KARG(5) : KARG(2);
  e.hb0 = __hip_atomic_load((gu32*)(e.ws + WS_FL), RLX_AGENT);
  SETK();
  DMA_WQK(1, 0); DMA_WQK(1, 1); DMA_X(1, 0); DMA_X(1, 1); DMA_X(1, 2); DMA_X(1, 3); DMA_WV(gw, 8192);
  SETK();
  DMA_WQK(2, 0); DMA_WQK(2, 1); DMA_X(2, 0); DMA_X(2, 1); DMA_X(2, 2); DMA_X(2, 3);
  e.sx = sx; e.sw = sw; e.vx0 = vx0; e.vx1 = vx1; e.vw = vw; e.gx0 = gx0; e.gw0 = gw0; e.k3 = k3; e.gw = gw; e.dx = dx; e.dqk = dqk; e.dv = dv ^ 8192u; e.wlo = isB ? ka[8] : ka[2];
  if (isB) body<true>(e, lds); else body<false>(e, lds);
}

extern "C" void kernel_launch(void* const* d_in, const int* in_sizes, int n_in, void* d_out, int out_size, void* d_ws, size_t ws_size, hipStream_t stream) {
  (void)in_sizes; (void)n_in; (void)out_size; (void)ws_size;
  (void)hipFuncSetAttribute((const void*)k_fused, hipFuncAttributeMaxDynamicSharedMemorySize, LDS_BYTES);
  k_fused<<<256, 512, LDS_BYTES, stream>>>((const char*)d_in[0], (const char*)d_in[1], (const char*)d_in[2], (const char*)d_in[3], (const char*)d_in[4], (const char*)d_in[5],
                                           (char*)d_ws, (char*)d_out);
}
```
